```python
import torch

def _softmax(x, axis=-1):
    return torch.softmax(x, dim=axis)
def _gelu(x):
    return torch.nn.functional.gelu(x, approximate="tanh")
def _silu(x):
    return torch.nn.functional.silu(x)
def _sigmoid(x):
    return torch.sigmoid(x)
def _layernorm(x, axis=-1, eps=1e-5):
    m = x.mean(dim=axis, keepdim=True)
    v = x.var(dim=axis, keepdim=True, unbiased=False)
    return (x - m) / torch.sqrt(v + eps)
def _scatter_add(tgt, idx, src, axis=0):
    return tgt.clone().index_add_(axis, idx, src)
def _segment_sum(src, seg, n):
    out = torch.zeros((n,) + src.shape[1:], dtype=src.dtype)
    return out.index_add_(0, seg, src)
def _pad(x, pw):
    flat = tuple(v for pair in reversed(pw) for v in pair)
    return torch.nn.functional.pad(x, flat)

kernel_name = 'sequential_preLN__linear_attn_block__dense_activation_silu__prompt'

def setup_inputs():

    _w = (lambda a: (lambda t: t.to(torch.bfloat16) if t.is_floating_point() else t)(torch.as_tensor(a)))
    def _A_setup():
        B, S, D = 1, 2048, 512
        return {
            "x": _w((lambda *s: torch.randn(*s, dtype=torch.bfloat16) * 0.05)(B, S, D)),
            "wqkv": _w((lambda *s: torch.randn(*s, dtype=torch.bfloat16) * 0.05)(D, 3 * D)),
            "wo": _w((lambda *s: torch.randn(*s, dtype=torch.bfloat16) * 0.05)(D, D)),
        }
    _A_in = _A_setup()

    def _B_setup():
        B, S, D, F = 1, 2048, 512, 1792
        return {
            "x": _w((lambda *s: torch.randn(*s, dtype=torch.bfloat16) * 0.05)(B, S, D)),
            "w1": _w((lambda *s: torch.randn(*s, dtype=torch.bfloat16) * 0.8)(D, F) * (D ** -0.5)),
            "w2": _w((lambda *s: torch.randn(*s, dtype=torch.bfloat16) * 0.1)(F, D) * (F ** -0.5)),
        }
    _B_in = _B_setup()

    return {
        "A_x": _A_in["x"],
        "A_wqkv": _A_in["wqkv"],
        "A_wo": _A_in["wo"],
        "B_x": _B_in["x"],
        "B_w1": _B_in["w1"],
        "B_w2": _B_in["w2"],
    }

def reference(A_x, A_wqkv, A_wo, B_x, B_w1, B_w2):
    def _A_ref(x, wqkv, wo):
        B, S, D = x.shape
        H = 32; Dh = D // H
        qkv = (lambda x, *d: x.permute(*d))((x @ wqkv).reshape(B, S, 3, H, Dh), 2, 0, 3, 1, 4)
        q, k, v = qkv[0], qkv[1], qkv[2]
        phi = lambda t: torch.where(t > 0, t + 1, torch.exp(t)) + 1e-6
        pq, pk = phi(q), phi(k)
        kv = torch.einsum("bhsd,bhse->bhde", pk, v)
        z = torch.einsum("bhsd->bhd", pk)
        num = torch.einsum("bhqd,bhde->bhqe", pq, kv)
        den = torch.einsum("bhqd,bhd->bhq", pq, z)[..., None] + 1e-6
        o = num / den
        return (lambda x, *d: x.permute(*d))(o, 0, 2, 1, 3).reshape(B, S, D) @ wo

    def _B_ref(x, w1, w2):
        h = torch.einsum("bsd,df->bsf", x, w1)
        h = _silu(h)
        out = torch.einsum("bsf,fd->bsd", h, w2)
        return out

    x = A_x
    h = x + _A_ref(_layernorm(x, axis=-1), A_wqkv, A_wo)
    h2 = h + _B_ref(_layernorm(h, axis=-1), B_w1, B_w2)
    return h2
```

```cpp
#include <hip/hip_runtime.h>
#include <hip/hip_bf16.h>
#include <stdint.h>

typedef unsigned short u16;
typedef __attribute__((ext_vector_type(8))) short bf16x8;
typedef __attribute__((ext_vector_type(4))) short s16x4;
typedef __attribute__((ext_vector_type(4))) float f32x4;
typedef __attribute__((ext_vector_type(4))) unsigned u32x4;
typedef short v4i16_t __attribute__((ext_vector_type(4)));

#define DEVI __device__ __forceinline__

DEVI float bf2f(u16 u) { return __uint_as_float(((unsigned)u) << 16); }
DEVI u16 f2bf(float f) {
  unsigned u = __float_as_uint(f);
  u += 0x7fffu + ((u >> 16) & 1u);
  return (u16)(u >> 16);
}
DEVI s16x4 trread(const u16* p) {
  return __builtin_amdgcn_ds_read_tr16_b64_v4i16((__attribute__((address_space(3))) v4i16_t*)p);
}
DEVI bf16x8 ldsB(const u16* sW, int ldw, int k0, int n0, int l) {
  const int g = l >> 4, q = (l & 15) >> 2, p = l & 3;
  const u16* a0 = sW + (k0 + 8 * g + q) * ldw + n0 + 4 * p;
  s16x4 lo = trread(a0);
  s16x4 hi = trread(a0 + 4 * ldw);
  bf16x8 r;
  r[0] = lo[0]; r[1] = lo[1]; r[2] = lo[2]; r[3] = lo[3];
  r[4] = hi[0]; r[5] = hi[1]; r[6] = hi[2]; r[7] = hi[3];
  return r;
}
DEVI float phi(float t) { return (t > 0.f ? t + 1.f : __expf(t)) + 1e-6f; }

#define S_LEN 2048
#define D_MODEL 512
#define F_DIM 1792

__global__ __launch_bounds__(256) void k1_ln_qkv(const u16* __restrict__ x, const u16* __restrict__ w,
                                                 u16* __restrict__ pq, float* __restrict__ kvacc) {
  constexpr int LDW = 104;
  __shared__ __attribute__((aligned(16))) u16 sW[512 * LDW];
  __shared__ float sRed[4][2][272];
  const int tid = threadIdx.x, l = tid & 63, wv = tid >> 6, g = l >> 4, c16 = l & 15;
  const int bid = blockIdx.x;
  const int mt = bid >> 4, hp = bid & 15;
#pragma unroll
  for (int i = 0; i < 24; ++i) {
    int idx = tid + 256 * i; int row = idx / 12; int c = idx - row * 12; int seg = c >> 2, cc = c & 3;
    u32x4 v = *(const u32x4*)(w + row * 1536 + seg * 512 + hp * 32 + cc * 8);
    *(u32x4*)(sW + row * LDW + seg * 32 + cc * 8) = v;
  }
  bf16x8 a[2][16];
#pragma unroll
  for (int ms = 0; ms < 2; ++ms)
#pragma unroll
    for (int ks = 0; ks < 16; ++ks)
      a[ms][ks] = *(const bf16x8*)(x + (mt * 128 + wv * 32 + ms * 16 + c16) * 512 + ks * 32 + g * 8);
#pragma unroll
  for (int ms = 0; ms < 2; ++ms) {
    float s = 0.f;
#pragma unroll
    for (int ks = 0; ks < 16; ++ks)
#pragma unroll
      for (int j = 0; j < 8; ++j) s += bf2f((u16)a[ms][ks][j]);
    s += __shfl_xor(s, 16); s += __shfl_xor(s, 32);
    const float mean = s * (1.f / 512.f);
    float ss = 0.f;
#pragma unroll
    for (int ks = 0; ks < 16; ++ks)
#pragma unroll
      for (int j = 0; j < 8; ++j) { float d = bf2f((u16)a[ms][ks][j]) - mean; ss += d * d; }
    ss += __shfl_xor(ss, 16); ss += __shfl_xor(ss, 32);
    const float rstd = rsqrtf(ss * (1.f / 512.f) + 1e-5f);
#pragma unroll
    for (int ks = 0; ks < 16; ++ks)
#pragma unroll
      for (int j = 0; j < 8; ++j) a[ms][ks][j] = (short)f2bf((bf2f((u16)a[ms][ks][j]) - mean) * rstd);
  }
  __syncthreads();
  f32x4 acc[2][6];
#pragma unroll
  for (int ms = 0; ms < 2; ++ms)
#pragma unroll
    for (int nt = 0; nt < 6; ++nt) acc[ms][nt] = (f32x4){0.f, 0.f, 0.f, 0.f};
#pragma unroll
  for (int ks = 0; ks < 16; ++ks) {
#pragma unroll
    for (int nt = 0; nt < 6; ++nt) {
      bf16x8 b = ldsB(sW, LDW, ks * 32, nt * 16, l);
#pragma unroll
      for (int ms = 0; ms < 2; ++ms)
        acc[ms][nt] = __builtin_amdgcn_mfma_f32_16x16x32_bf16(a[ms][ks], b, acc[ms][nt], 0, 0, 0);
    }
  }
#pragma unroll
  for (int ms = 0; ms < 2; ++ms)
#pragma unroll
    for (int nt = 0; nt < 2; ++nt)
#pragma unroll
      for (int r = 0; r < 4; ++r) {
        float t = bf2f(f2bf(acc[ms][nt][r]));
        pq[(mt * 128 + wv * 32 + ms * 16 + 4 * g + r) * 512 + hp * 32 + nt * 16 + c16] = f2bf(phi(t));
      }
#pragma unroll
  for (int hh = 0; hh < 2; ++hh) {
    bf16x8 pk, vv; float zs = 0.f;
#pragma unroll
    for (int ms = 0; ms < 2; ++ms)
#pragma unroll
      for (int r = 0; r < 4; ++r) {
        float t = bf2f(f2bf(acc[ms][2 + hh][r]));
        u16 pb = f2bf(phi(t));
        zs += bf2f(pb);
        pk[ms * 4 + r] = (short)pb;
        vv[ms * 4 + r] = (short)f2bf(acc[ms][4 + hh][r]);
      }
    f32x4 kvt = __builtin_amdgcn_mfma_f32_16x16x32_bf16(pk, vv, (f32x4){0.f, 0.f, 0.f, 0.f}, 0, 0, 0);
    zs += __shfl_xor(zs, 16); zs += __shfl_xor(zs, 32);
#pragma unroll
    for (int r = 0; r < 4; ++r) sRed[wv][hh][(4 * g + r) * 16 + c16] = kvt[r];
    if (g == 0) sRed[wv][hh][256 + c16] = zs;
  }
  __syncthreads();
  for (int i = tid; i < 544; i += 256) {
    int hh = i / 272, e = i - hh * 272;
    float s = sRed[0][hh][e] + sRed[1][hh][e] + sRed[2][hh][e] + sRed[3][hh][e];
    atomicAdd(kvacc + (hp * 2 + hh) * 272 + e, s);
  }
}

__global__ __launch_bounds__(256) void k2_attn_out(const u16* __restrict__ x, const u16* __restrict__ pq,
                                                   const float* __restrict__ kvacc, const u16* __restrict__ wo,
                                                   u16* __restrict__ h) {
  constexpr int LDA = 520, LDW = 72;
  __shared__ __attribute__((aligned(16))) u16 sA[64 * LDA];
  __shared__ __attribute__((aligned(16))) u16 sW[512 * LDW];
  const int tid = threadIdx.x, l = tid & 63, wv = tid >> 6, g = l >> 4, c16 = l & 15;
  const int bid = blockIdx.x;
  const int mt = bid >> 3, nt0 = (bid & 7) * 64;
#pragma unroll
  for (int i = 0; i < 16; ++i) {
    int idx = tid + 256 * i; int row = idx >> 3, cc = idx & 7;
    u32x4 v = *(const u32x4*)(wo + row * 512 + nt0 + cc * 8);
    *(u32x4*)(sW + row * LDW + cc * 8) = v;
  }
  const int r0 = mt * 64 + wv * 16;
#pragma unroll 4
  for (int hd = 0; hd < 32; ++hd) {
    s16x4 af = *(const s16x4*)(pq + (r0 + c16) * 512 + hd * 16 + 4 * g);
    s16x4 bfr, bz;
#pragma unroll
    for (int j = 0; j < 4; ++j) {
      bfr[j] = (short)f2bf(kvacc[hd * 272 + (4 * g + j) * 16 + c16]);
      bz[j] = (short)f2bf(kvacc[hd * 272 + 256 + 4 * g + j]);
    }
    f32x4 num = __builtin_amdgcn_mfma_f32_16x16x16bf16_1k(af, bfr, (f32x4){0.f, 0.f, 0.f, 0.f}, 0, 0, 0);
    f32x4 den = __builtin_amdgcn_mfma_f32_16x16x16bf16_1k(af, bz, (f32x4){0.f, 0.f, 0.f, 0.f}, 0, 0, 0);
#pragma unroll
    for (int r = 0; r < 4; ++r)
      sA[(wv * 16 + 4 * g + r) * LDA + hd * 16 + c16] = f2bf(num[r] / (den[r] + 1e-6f));
  }
  __syncthreads();
  const int wr = (wv >> 1) * 32, wc = (wv & 1) * 32;
  f32x4 acc[2][2];
#pragma unroll
  for (int i = 0; i < 2; ++i)
#pragma unroll
    for (int j = 0; j < 2; ++j) acc[i][j] = (f32x4){0.f, 0.f, 0.f, 0.f};
#pragma unroll
  for (int ks = 0; ks < 16; ++ks) {
    bf16x8 af[2], bfr[2];
#pragma unroll
    for (int i = 0; i < 2; ++i) af[i] = *(const bf16x8*)(sA + (wr + i * 16 + c16) * LDA + ks * 32 + g * 8);
#pragma unroll
    for (int j = 0; j < 2; ++j) bfr[j] = ldsB(sW, LDW, ks * 32, wc + j * 16, l);
#pragma unroll
    for (int i = 0; i < 2; ++i)
#pragma unroll
      for (int j = 0; j < 2; ++j)
        acc[i][j] = __builtin_amdgcn_mfma_f32_16x16x32_bf16(af[i], bfr[j], acc[i][j], 0, 0, 0);
  }
#pragma unroll
  for (int i = 0; i < 2; ++i)
#pragma unroll
    for (int j = 0; j < 2; ++j)
#pragma unroll
      for (int r = 0; r < 4; ++r) {
        int row = mt * 64 + wr + i * 16 + 4 * g + r, col = nt0 + wc + j * 16 + c16;
        float att = bf2f(f2bf(acc[i][j][r]));
        h[row * 512 + col] = f2bf(bf2f(x[row * 512 + col]) + att);
      }
}

__global__ __launch_bounds__(256) void k3_ln_w1(const u16* __restrict__ h, const u16* __restrict__ w,
                                                u16* __restrict__ h1) {
  constexpr int LDW = 120;
  __shared__ __attribute__((aligned(16))) u16 sW[512 * LDW];
  const int tid = threadIdx.x, l = tid & 63, wv = tid >> 6, g = l >> 4, c16 = l & 15;
  const int bid = blockIdx.x;
  const int mt = bid >> 4, n0 = (bid & 15) * 112;
#pragma unroll
  for (int i = 0; i < 28; ++i) {
    int idx = tid + 256 * i; int row = idx / 14; int cc = idx - row * 14;
    u32x4 v = *(const u32x4*)(w + row * F_DIM + n0 + cc * 8);
    *(u32x4*)(sW + row * LDW + cc * 8) = v;
  }
  bf16x8 a[2][16];
#pragma unroll
  for (int ms = 0; ms < 2; ++ms)
#pragma unroll
    for (int ks = 0; ks < 16; ++ks)
      a[ms][ks] = *(const bf16x8*)(h + (mt * 128 + wv * 32 + ms * 16 + c16) * 512 + ks * 32 + g * 8);
#pragma unroll
  for (int ms = 0; ms < 2; ++ms) {
    float s = 0.f;
#pragma unroll
    for (int ks = 0; ks < 16; ++ks)
#pragma unroll
      for (int j = 0; j < 8; ++j) s += bf2f((u16)a[ms][ks][j]);
    s += __shfl_xor(s, 16); s += __shfl_xor(s, 32);
    const float mean = s * (1.f / 512.f);
    float ss = 0.f;
#pragma unroll
    for (int ks = 0; ks < 16; ++ks)
#pragma unroll
      for (int j = 0; j < 8; ++j) { float d = bf2f((u16)a[ms][ks][j]) - mean; ss += d * d; }
    ss += __shfl_xor(ss, 16); ss += __shfl_xor(ss, 32);
    const float rstd = rsqrtf(ss * (1.f / 512.f) + 1e-5f);
#pragma unroll
    for (int ks = 0; ks < 16; ++ks)
#pragma unroll
      for (int j = 0; j < 8; ++j) a[ms][ks][j] = (short)f2bf((bf2f((u16)a[ms][ks][j]) - mean) * rstd);
  }
  __syncthreads();
  f32x4 acc[2][7];
#pragma unroll
  for (int ms = 0; ms < 2; ++ms)
#pragma unroll
    for (int nt = 0; nt < 7; ++nt) acc[ms][nt] = (f32x4){0.f, 0.f, 0.f, 0.f};
#pragma unroll
  for (int ks = 0; ks < 16; ++ks) {
#pragma unroll
    for (int nt = 0; nt < 7; ++nt) {
      bf16x8 b = ldsB(sW, LDW, ks * 32, nt * 16, l);
#pragma unroll
      for (int ms = 0; ms < 2; ++ms)
        acc[ms][nt] = __builtin_amdgcn_mfma_f32_16x16x32_bf16(a[ms][ks], b, acc[ms][nt], 0, 0, 0);
    }
  }
#pragma unroll
  for (int ms = 0; ms < 2; ++ms)
#pragma unroll
    for (int nt = 0; nt < 7; ++nt)
#pragma unroll
      for (int r = 0; r < 4; ++r) {
        float t = bf2f(f2bf(acc[ms][nt][r]));
        float sv = t / (1.f + __expf(-t));
        h1[(mt * 128 + wv * 32 + ms * 16 + 4 * g + r) * F_DIM + n0 + nt * 16 + c16] = f2bf(sv);
      }
}

__global__ __launch_bounds__(256) void k4_w2(const u16* __restrict__ h, const u16* __restrict__ h1,
                                             const u16* __restrict__ w2, u16* __restrict__ out) {
  constexpr int LDA = 264, LDW = 72;
  __shared__ __attribute__((aligned(16))) u16 sA[2][64 * LDA];
  __shared__ __attribute__((aligned(16))) u16 sW[2][256 * LDW];
  const int tid = threadIdx.x, l = tid & 63, wv = tid >> 6, g = l >> 4, c16 = l & 15;
  const int bid = blockIdx.x;
  const int mt = bid >> 3, nt0 = (bid & 7) * 64;
  u32x4 ra[8], rw[8];
  auto gload = [&](int ch) {
#pragma unroll
    for (int i = 0; i < 8; ++i) {
      int idx = tid + 256 * i; int row = idx >> 5, cc = idx & 31;
      ra[i] = *(const u32x4*)(h1 + (mt * 64 + row) * F_DIM + ch * 256 + cc * 8);
    }
#pragma unroll
    for (int i = 0; i < 8; ++i) {
      int idx = tid + 256 * i; int row = idx >> 3, cc = idx & 7;
      rw[i] = *(const u32x4*)(w2 + (ch * 256 + row) * 512 + nt0 + cc * 8);
    }
  };
  auto swrite = [&](int buf) {
#pragma unroll
    for (int i = 0; i < 8; ++i) {
      int idx = tid + 256 * i; int row = idx >> 5, cc = idx & 31;
      *(u32x4*)(sA[buf] + row * LDA + cc * 8) = ra[i];
    }
#pragma unroll
    for (int i = 0; i < 8; ++i) {
      int idx = tid + 256 * i; int row = idx >> 3, cc = idx & 7;
      *(u32x4*)(sW[buf] + row * LDW + cc * 8) = rw[i];
    }
  };
  const int wr = (wv >> 1) * 32, wc = (wv & 1) * 32;
  f32x4 acc[2][2];
#pragma unroll
  for (int i = 0; i < 2; ++i)
#pragma unroll
    for (int j = 0; j < 2; ++j) acc[i][j] = (f32x4){0.f, 0.f, 0.f, 0.f};
  gload(0);
  swrite(0);
  __syncthreads();
  for (int ch = 0; ch < 7; ++ch) {
    const int buf = ch & 1;
    if (ch + 1 < 7) gload(ch + 1);
#pragma unroll
    for (int ks = 0; ks < 8; ++ks) {
      bf16x8 af[2], bfr[2];
#pragma unroll
      for (int i = 0; i < 2; ++i) af[i] = *(const bf16x8*)(sA[buf] + (wr + i * 16 + c16) * LDA + ks * 32 + g * 8);
#pragma unroll
      for (int j = 0; j < 2; ++j) bfr[j] = ldsB(sW[buf], LDW, ks * 32, wc + j * 16, l);
#pragma unroll
      for (int i = 0; i < 2; ++i)
#pragma unroll
        for (int j = 0; j < 2; ++j)
          acc[i][j] = __builtin_amdgcn_mfma_f32_16x16x32_bf16(af[i], bfr[j], acc[i][j], 0, 0, 0);
    }
    if (ch + 1 < 7) swrite(buf ^ 1);
    __syncthreads();
  }
#pragma unroll
  for (int i = 0; i < 2; ++i)
#pragma unroll
    for (int j = 0; j < 2; ++j)
#pragma unroll
      for (int r = 0; r < 4; ++r) {
        int row = mt * 64 + wr + i * 16 + 4 * g + r, col = nt0 + wc + j * 16 + c16;
        float o = bf2f(f2bf(acc[i][j][r]));
        out[row * 512 + col] = f2bf(bf2f(h[row * 512 + col]) + o);
      }
}

extern "C" void kernel_launch(void* const* d_in, const int* in_sizes, int n_in,
                              void* d_out, int out_size, void* d_ws, size_t ws_size,
                              hipStream_t stream) {
  const u16* x = (const u16*)d_in[0];
  const u16* wqkv = (const u16*)d_in[1];
  const u16* wo = (const u16*)d_in[2];
  const u16* w1 = (const u16*)d_in[4];
  const u16* w2 = (const u16*)d_in[5];
  char* ws = (char*)d_ws;
  float* kvacc = (float*)ws;
  u16* pq = (u16*)(ws + 65536);
  u16* h = (u16*)(ws + 65536 + 2097152);
  u16* h1 = (u16*)(ws + 65536 + 2 * 2097152);
  hipMemsetAsync(kvacc, 0, 32 * 272 * 4, stream);
  k1_ln_qkv<<<256, 256, 0, stream>>>(x, wqkv, pq, kvacc);
  k2_attn_out<<<256, 256, 0, stream>>>(x, pq, kvacc, wo, h);
  k3_ln_w1<<<256, 256, 0, stream>>>(h, w1, h1);
  k4_w2<<<256, 256, 0, stream>>>(h, h1, w2, (u16*)d_out);
}
```

```cpp
#include <hip/hip_runtime.h>
#include <hip/hip_bf16.h>
#include <stdint.h>

typedef unsigned short u16;
typedef __attribute__((ext_vector_type(8))) short bf16x8;
typedef __attribute__((ext_vector_type(4))) short s16x4;
typedef __attribute__((ext_vector_type(4))) float f32x4;
typedef __attribute__((ext_vector_type(4))) unsigned u32x4;
typedef short v4i16_t __attribute__((ext_vector_type(4)));

#define DEVI __device__ __forceinline__

DEVI float bf2f(u16 u) { return __uint_as_float(((unsigned)u) << 16); }
DEVI u16 f2bf(float f) {
  unsigned u = __float_as_uint(f);
  u += 0x7fffu + ((u >> 16) & 1u);
  return (u16)(u >> 16);
}
DEVI s16x4 trread(const u16* p) {
  return __builtin_amdgcn_ds_read_tr16_b64_v4i16((__attribute__((address_space(3))) v4i16_t*)p);
}
DEVI bf16x8 ldsB(const u16* sW, int ldw, int k0, int n0, int l) {
  const int g = l >> 4, q = (l & 15) >> 2, p = l & 3;
  const u16* a0 = sW + (k0 + 8 * g + q) * ldw + n0 + 4 * p;
  s16x4 lo = trread(a0);
  s16x4 hi = trread(a0 + 4 * ldw);
  bf16x8 r;
  r[0] = lo[0]; r[1] = lo[1]; r[2] = lo[2]; r[3] = lo[3];
  r[4] = hi[0]; r[5] = hi[1]; r[6] = hi[2]; r[7] = hi[3];
  return r;
}
DEVI float phi(float t) { return (t > 0.f ? t + 1.f : __expf(t)) + 1e-6f; }

#define S_LEN 2048
#define D_MODEL 512
#define F_DIM 1792


typedef __attribute__((ext_vector_type(2))) unsigned u32x2;
DEVI void ln16(bf16x8 (&a)[16]) {
  float s = 0.f;
#pragma unroll
  for (int ks = 0; ks < 16; ++ks) {
    asm volatile("" : "+v"(a[ks]), "+v"(s));
#pragma unroll
    for (int j = 0; j < 8; ++j) s += bf2f((u16)a[ks][j]);
  }
  s += __shfl_xor(s, 16); s += __shfl_xor(s, 32);
  const float mean = s * (1.f / 512.f);
  asm volatile("" ::: "memory");
  float ss = 0.f;
#pragma unroll
  for (int ks = 0; ks < 16; ++ks) {
    asm volatile("" : "+v"(a[ks]), "+v"(ss));
#pragma unroll
    for (int j = 0; j < 8; ++j) { float d = bf2f((u16)a[ks][j]) - mean; ss += d * d; }
  }
  ss += __shfl_xor(ss, 16); ss += __shfl_xor(ss, 32);
  const float rstd = rsqrtf(ss * (1.f / 512.f) + 1e-5f);
#pragma unroll
  for (int ks = 0; ks < 16; ++ks) {
    asm volatile("" : "+v"(a[ks]));
#pragma unroll
    for (int j = 0; j < 8; ++j) a[ks][j] = (short)f2bf((bf2f((u16)a[ks][j]) - mean) * rstd);
  }
}
DEVI u32x2 pack4(float a, float b, float c, float d) {
  u32x2 r; r[0] = f2bf(a) | ((unsigned)f2bf(b) << 16); r[1] = f2bf(c) | ((unsigned)f2bf(d) << 16); return r;
}

__global__ __launch_bounds__(512) void k1_ln_qkv(const u16* __restrict__ x, const u16* __restrict__ w,
                                                  u16* __restrict__ pq, float* __restrict__ kvacc) {
  constexpr int LDW = 104;
  __shared__ __attribute__((aligned(16))) u16 sW[512 * LDW];
  __shared__ float sRed[8][2][272];
  const int tid = threadIdx.x, l = tid & 63, wv = tid >> 6, g = l >> 4, c16 = l & 15;
  const int bid = blockIdx.x, mt = bid >> 4, hp = bid & 15;
  const int wm = wv;
  u32x4 wr_[12];
#pragma unroll
  for (int i = 0; i < 12; ++i) {
    int idx = tid + 512 * i; int row = idx / 12; int c = idx - row * 12;
    wr_[i] = *(const u32x4*)(w + row * 1536 + (c >> 2) * 512 + hp * 32 + (c & 3) * 8);
  }
  bf16x8 a[16];
  const int row0 = mt * 128 + wm * 16;
#pragma unroll
  for (int ks = 0; ks < 16; ++ks) a[ks] = *(const bf16x8*)(x + (row0 + c16) * 512 + ks * 32 + g * 8);
#pragma unroll
  for (int i = 0; i < 12; ++i) {
    int idx = tid + 512 * i; int row = idx / 12; int c = idx - row * 12;
    *(u32x4*)(sW + row * LDW + (c >> 2) * 32 + (c & 3) * 8) = wr_[i];
  }
  ln16(a);
  __syncthreads();
#pragma unroll
  for (int hh = 0; hh < 2; ++hh) {
  f32x4 aq = {0.f, 0.f, 0.f, 0.f}, ak = aq, av = aq;
#pragma unroll
  for (int ks = 0; ks < 16; ++ks) {
    bf16x8 bq = ldsB(sW, LDW, ks * 32, hh * 16, l);
    bf16x8 bk = ldsB(sW, LDW, ks * 32, 32 + hh * 16, l);
    bf16x8 bv = ldsB(sW, LDW, ks * 32, 64 + hh * 16, l);
    aq = __builtin_amdgcn_mfma_f32_16x16x32_bf16(bq, a[ks], aq, 0, 0, 0);
    ak = __builtin_amdgcn_mfma_f32_16x16x32_bf16(a[ks], bk, ak, 0, 0, 0);
    av = __builtin_amdgcn_mfma_f32_16x16x32_bf16(a[ks], bv, av, 0, 0, 0);
  }
  *(u32x2*)(pq + (row0 + c16) * 512 + hp * 32 + hh * 16 + 4 * g) =
      pack4(phi(bf2f(f2bf(aq[0]))), phi(bf2f(f2bf(aq[1]))), phi(bf2f(f2bf(aq[2]))), phi(bf2f(f2bf(aq[3]))));
  {
    s16x4 pk, vv; float zs = 0.f;
#pragma unroll
    for (int r = 0; r < 4; ++r) {
      u16 pb = f2bf(phi(bf2f(f2bf(ak[r]))));
      zs += bf2f(pb); pk[r] = (short)pb; vv[r] = (short)f2bf(av[r]);
    }
    f32x4 kvt = __builtin_amdgcn_mfma_f32_16x16x16bf16_1k(pk, vv, (f32x4){0.f, 0.f, 0.f, 0.f}, 0, 0, 0);
    zs += __shfl_xor(zs, 16); zs += __shfl_xor(zs, 32);
#pragma unroll
    for (int r = 0; r < 4; ++r) sRed[wm][hh][(4 * g + r) * 16 + c16] = kvt[r];
    if (g == 0) sRed[wm][hh][256 + c16] = zs;
  }
  }
  __syncthreads();
  for (int t2 = tid; t2 < 544; t2 += 512) {
    int h2 = t2 / 272, e = t2 - h2 * 272;
    float s = 0.f;
#pragma unroll
    for (int i = 0; i < 8; ++i) s += sRed[i][h2][e];
    atomicAdd(kvacc + (hp * 2 + h2) * 272 + e, s);
  }
}

__global__ __launch_bounds__(1024) void k2_attn_out(const u16* __restrict__ x, const u16* __restrict__ pq,
                                                    const float* __restrict__ kvacc, const u16* __restrict__ wo,
                                                    u16* __restrict__ h) {
  constexpr int LDA = 520, LDW = 72;
  __shared__ __attribute__((aligned(16))) u16 sA[64 * LDA];
  __shared__ __attribute__((aligned(16))) u16 sW[512 * LDW];
  const int tid = threadIdx.x, l = tid & 63, wv = tid >> 6, g = l >> 4, c16 = l & 15;
  const int bid = blockIdx.x, mt = bid >> 3, nt0 = (bid & 7) * 64;
  u32x4 wr_[4];
#pragma unroll
  for (int i = 0; i < 4; ++i) { int idx = tid + 1024 * i; wr_[i] = *(const u32x4*)(wo + (idx >> 3) * 512 + nt0 + (idx & 7) * 8); }
  const int rg = wv & 3, r0 = mt * 64 + rg * 16;
  s16x4 af[8]; float kvv[8][4], zz[8][4];
#pragma unroll
  for (int i = 0; i < 8; ++i) {
    int hd = (wv >> 2) * 8 + i;
    af[i] = *(const s16x4*)(pq + (r0 + c16) * 512 + hd * 16 + 4 * g);
#pragma unroll
    for (int j = 0; j < 4; ++j) { kvv[i][j] = kvacc[hd * 272 + (4 * g + j) * 16 + c16]; zz[i][j] = kvacc[hd * 272 + 256 + 4 * g + j]; }
  }
#pragma unroll
  for (int i = 0; i < 4; ++i) { int idx = tid + 1024 * i; *(u32x4*)(sW + (idx >> 3) * LDW + (idx & 7) * 8) = wr_[i]; }
#pragma unroll
  for (int i = 0; i < 8; ++i) {
    int hd = (wv >> 2) * 8 + i;
    s16x4 bfr, bz;
#pragma unroll
    for (int j = 0; j < 4; ++j) { bfr[j] = (short)f2bf(kvv[i][j]); bz[j] = (short)f2bf(zz[i][j]); }
    f32x4 num = __builtin_amdgcn_mfma_f32_16x16x16bf16_1k(af[i], bfr, (f32x4){0.f, 0.f, 0.f, 0.f}, 0, 0, 0);
    f32x4 den = __builtin_amdgcn_mfma_f32_16x16x16bf16_1k(af[i], bz, (f32x4){0.f, 0.f, 0.f, 0.f}, 0, 0, 0);
#pragma unroll
    for (int r = 0; r < 4; ++r) sA[(rg * 16 + 4 * g + r) * LDA + hd * 16 + c16] = f2bf(num[r] / (den[r] + 1e-6f));
  }
  __syncthreads();
  const int wr = (wv & 3) * 16, wc = (wv >> 2) * 16;
  f32x4 acc = {0.f, 0.f, 0.f, 0.f};
#pragma unroll
  for (int ks = 0; ks < 16; ++ks) {
    bf16x8 a = *(const bf16x8*)(sA + (wr + c16) * LDA + ks * 32 + g * 8);
    bf16x8 b = ldsB(sW, LDW, ks * 32, wc, l);
    acc = __builtin_amdgcn_mfma_f32_16x16x32_bf16(b, a, acc, 0, 0, 0);
  }
  const int off = (mt * 64 + wr + c16) * 512 + nt0 + wc + 4 * g;
  u32x2 xv = *(const u32x2*)(x + off);
  *(u32x2*)(h + off) = pack4(bf2f(xv[0] & 0xffff) + bf2f(f2bf(acc[0])), bf2f(xv[0] >> 16) + bf2f(f2bf(acc[1])),
                             bf2f(xv[1] & 0xffff) + bf2f(f2bf(acc[2])), bf2f(xv[1] >> 16) + bf2f(f2bf(acc[3])));
}

__global__ __launch_bounds__(512) void k3_ln_w1(const u16* __restrict__ h, const u16* __restrict__ w,
                                                 u16* __restrict__ h1) {
  constexpr int LDW = 136;
  __shared__ __attribute__((aligned(16))) u16 sW[512 * LDW];
  const int tid = threadIdx.x, l = tid & 63, wv = tid >> 6, g = l >> 4, c16 = l & 15;
  const int bid = blockIdx.x, mt = bid / 14, n0 = (bid - mt * 14) * 128;
  const int wm = wv; const int wn = 0;
  u32x4 wr_[16];
#pragma unroll
  for (int i = 0; i < 16; ++i) { int idx = tid + 512 * i; wr_[i] = *(const u32x4*)(w + (idx >> 4) * F_DIM + n0 + (idx & 15) * 8); }
  bf16x8 a[16];
  const int row0 = mt * 128 + wm * 16;
#pragma unroll
  for (int ks = 0; ks < 16; ++ks) a[ks] = *(const bf16x8*)(h + (row0 + c16) * 512 + ks * 32 + g * 8);
#pragma unroll
  for (int i = 0; i < 16; ++i) { int idx = tid + 512 * i; *(u32x4*)(sW + (idx >> 4) * LDW + (idx & 15) * 8) = wr_[i]; }
  ln16(a);
  __syncthreads();
  f32x4 acc[8];
#pragma unroll
  for (int nt = 0; nt < 8; ++nt) acc[nt] = (f32x4){0.f, 0.f, 0.f, 0.f};
#pragma unroll
  for (int ks = 0; ks < 16; ++ks)
#pragma unroll
    for (int nt = 0; nt < 8; ++nt)
      acc[nt] = __builtin_amdgcn_mfma_f32_16x16x32_bf16(ldsB(sW, LDW, ks * 32, wn + nt * 16, l), a[ks], acc[nt], 0, 0, 0);
#pragma unroll
  for (int nt = 0; nt < 8; ++nt) {
    float o[4];
#pragma unroll
    for (int r = 0; r < 4; ++r) { float t = bf2f(f2bf(acc[nt][r])); o[r] = t / (1.f + __expf(-t)); }
    *(u32x2*)(h1 + (row0 + c16) * F_DIM + n0 + wn + nt * 16 + 4 * g) = pack4(o[0], o[1], o[2], o[3]);
  }
}

__global__ __launch_bounds__(1024) void k4_w2(const u16* __restrict__ h, const u16* __restrict__ h1,
                                              const u16* __restrict__ w2, u16* __restrict__ out) {
  constexpr int LDA = 264, LDW = 72;
  __shared__ __attribute__((aligned(16))) u16 sA[2][64 * LDA];
  __shared__ __attribute__((aligned(16))) u16 sW[2][256 * LDW];
  const int tid = threadIdx.x, l = tid & 63, wv = tid >> 6, g = l >> 4, c16 = l & 15;
  const int bid = blockIdx.x, mt = bid >> 3, nt0 = (bid & 7) * 64;
  u32x4 ra[2], rw[2];
  const int wr = (wv & 3) * 16, wc = (wv >> 2) * 16;
  f32x4 acc = {0.f, 0.f, 0.f, 0.f};
#define GL(ch) { _Pragma("unroll") for (int i = 0; i < 2; ++i) { int idx = tid + 1024 * i; \
      ra[i] = *(const u32x4*)(h1 + (mt * 64 + (idx >> 5)) * F_DIM + (ch) * 256 + (idx & 31) * 8); \
      rw[i] = *(const u32x4*)(w2 + ((ch) * 256 + (idx >> 3)) * 512 + nt0 + (idx & 7) * 8); } }
#define SW(buf) { _Pragma("unroll") for (int i = 0; i < 2; ++i) { int idx = tid + 1024 * i; \
      *(u32x4*)(sA[buf] + (idx >> 5) * LDA + (idx & 31) * 8) = ra[i]; \
      *(u32x4*)(sW[buf] + (idx >> 3) * LDW + (idx & 7) * 8) = rw[i]; } }
  GL(0) SW(0)
  __syncthreads();
  for (int ch = 0; ch < 7; ++ch) {
    const int buf = ch & 1;
    if (ch + 1 < 7) GL(ch + 1)
#pragma unroll
    for (int ks = 0; ks < 8; ++ks) {
      bf16x8 a = *(const bf16x8*)(sA[buf] + (wr + c16) * LDA + ks * 32 + g * 8);
      bf16x8 b = ldsB(sW[buf], LDW, ks * 32, wc, l);
      acc = __builtin_amdgcn_mfma_f32_16x16x32_bf16(b, a, acc, 0, 0, 0);
    }
    if (ch + 1 < 7) SW(buf ^ 1)
    __syncthreads();
  }
  const int off = (mt * 64 + wr + c16) * 512 + nt0 + wc + 4 * g;
  u32x2 hv = *(const u32x2*)(h + off);
  *(u32x2*)(out + off) = pack4(bf2f(hv[0] & 0xffff) + bf2f(f2bf(acc[0])), bf2f(hv[0] >> 16) + bf2f(f2bf(acc[1])),
                               bf2f(hv[1] & 0xffff) + bf2f(f2bf(acc[2])), bf2f(hv[1] >> 16) + bf2f(f2bf(acc[3])));
}

extern "C" void kernel_launch(void* const* d_in, const int* in_sizes, int n_in,
                              void* d_out, int out_size, void* d_ws, size_t ws_size,
                              hipStream_t stream) {
  const u16* x = (const u16*)d_in[0];
  const u16* wqkv = (const u16*)d_in[1];
  const u16* wo = (const u16*)d_in[2];
  const u16* w1 = (const u16*)d_in[4];
  const u16* w2 = (const u16*)d_in[5];
  char* ws = (char*)d_ws;
  float* kvacc = (float*)ws;
  u16* pq = (u16*)(ws + 65536);
  u16* h = (u16*)(ws + 65536 + 2097152);
  u16* h1 = (u16*)(ws + 65536 + 2 * 2097152);
  (void)hipMemsetAsync(kvacc, 0, 32 * 272 * 4, stream);
  k1_ln_qkv<<<256, 512, 0, stream>>>(x, wqkv, pq, kvacc);
  k2_attn_out<<<256, 1024, 0, stream>>>(x, pq, kvacc, wo, h);
  k3_ln_w1<<<224, 512, 0, stream>>>(h, w1, h1);
  k4_w2<<<256, 1024, 0, stream>>>(h, h1, w2, (u16*)d_out);
}
```
